```python
import torch

def _softmax(x, axis=-1):
    return torch.softmax(x, dim=axis)

def _gelu(x):
    return torch.nn.functional.gelu(x, approximate='tanh')

def _silu(x):
    return torch.nn.functional.silu(x)

def _sigmoid(x):
    return torch.sigmoid(x)

def _layernorm(x, axis=-1, eps=1e-05):
    m = x.mean(dim=axis, keepdim=True)
    v = x.var(dim=axis, keepdim=True, unbiased=False)
    return (x - m) / torch.sqrt(v + eps)

def _scatter_add(tgt, idx, src, axis=0):
    return tgt.clone().index_add_(axis, idx, src)

def _segment_sum(src, seg, n):
    out = torch.zeros((n,) + src.shape[1:], dtype=src.dtype)
    return out.index_add_(0, seg, src)

def _pad(x, pw):
    flat = tuple((v for pair in reversed(pw) for v in pair))
    return torch.nn.functional.pad(x, flat)
kernel_name = 'standalone__tree_speculative_verify_tree_attention_over_candidate_token_tree__decode'

def setup_inputs():

    _w = lambda a: (lambda t: t.to(torch.bfloat16) if t.is_floating_point() else t)(torch.as_tensor(a))
    B, Hq, Hkv, T, C, D = (2, 32, 16, 1, 16384, 128)
    tree_mask = torch.zeros((B, T, T), dtype=float)
    for b in range(B):
        parent = torch.zeros(T, dtype=torch.int64)
        for i in range(1, T):
            parent[i] = int(torch.randint(0, i, (1,)).item())
        for i in range(T):
            j = i
            tree_mask[b, i, j] = 1.0
            while j > 0:
                j = parent[j]
                tree_mask[b, i, j] = 1.0
    return {'q': _w((lambda *s: torch.randn(*s, dtype=torch.bfloat16) * 0.8)(B, Hq, T, D)), 'k_cache': _w((lambda *s: torch.randn(*s, dtype=torch.bfloat16) * 0.8)(B, Hkv, C, D)), 'v_cache': _w((lambda *s: torch.randn(*s, dtype=torch.bfloat16) * 0.05)(B, Hkv, C, D)), 'k_tree': _w((lambda *s: torch.randn(*s, dtype=torch.bfloat16) * 0.8)(B, Hkv, T, D)), 'v_tree': _w((lambda *s: torch.randn(*s, dtype=torch.bfloat16) * 0.05)(B, Hkv, T, D)), 'tree_mask': _w(tree_mask)}

def reference(q, k_cache, v_cache, k_tree, v_tree, tree_mask):
    B, Hq, T, D = q.shape
    Hkv, C = (k_cache.shape[1], k_cache.shape[2])
    rep = Hq // Hkv
    kc = (lambda x, n, axis: torch.repeat_interleave(x, n, dim=axis))(k_cache, rep, axis=1)
    vc = (lambda x, n, axis: torch.repeat_interleave(x, n, dim=axis))(v_cache, rep, axis=1)
    kt = (lambda x, n, axis: torch.repeat_interleave(x, n, dim=axis))(k_tree, rep, axis=1)
    vt = (lambda x, n, axis: torch.repeat_interleave(x, n, dim=axis))(v_tree, rep, axis=1)
    k = torch.concatenate([kc, kt], axis=2)
    v = torch.concatenate([vc, vt], axis=2)
    scale = D ** (-0.5)
    scores = torch.einsum('bhqd,bhkd->bhqk', q, k) * scale
    ctx_mask = torch.ones((B, 1, T, C), dtype=scores.dtype)
    tm = tree_mask[:, None, :, :].clone()
    full_mask = torch.concatenate([ctx_mask, tm], axis=-1)
    scores = scores + (1.0 - full_mask) * -1000000000.0
    p = _softmax(scores, axis=-1)
    return torch.einsum('bhqk,bhkd->bhqd', p, v)
```

```cpp
#include <hip/hip_runtime.h>
#include <hip/hip_bf16.h>
#include <cstdint>

namespace {

constexpr int B_ = 2, HQ = 32, HKV = 16, C_ = 16384, D_ = 128, G_ = HQ / HKV;
constexpr int NSPLIT = 8, WAVES = 4, DEPTH = 4;
constexpr int NTHR = WAVES * 64;
constexpr int TPS = C_ / NSPLIT;
constexpr int TPW = TPS / WAVES;
constexpr int NCH = TPW / 16;
static_assert(G_ == 2, "two query heads per kv head");
static_assert(TPW % 16 == 0 && NCH >= DEPTH, "chunking");
constexpr float SCALE = 0.088388347648318440f;
constexpr float LOG2E = 1.4426950408889634f;
constexpr float C2 = SCALE * LOG2E;

constexpr size_t WS_EPOCH = 0;
constexpr size_t WS_GRAN = 8192;
constexpr int GREC = D_ + 2;

using bf16x2 = __attribute__((ext_vector_type(2))) __bf16;
using u32x4 = __attribute__((ext_vector_type(4))) unsigned;
using f32x4 = __attribute__((ext_vector_type(4))) float;
using f32x2 = __attribute__((ext_vector_type(2))) float;

__device__ __forceinline__ float dot2(unsigned k, unsigned q, float acc) {
  return __builtin_amdgcn_fdot2_f32_bf16(__builtin_bit_cast(bf16x2, k), __builtin_bit_cast(bf16x2, q), acc, false);
}
template <int CTRL> __device__ __forceinline__ float dpp(float x) {
  return __builtin_bit_cast(float, __builtin_amdgcn_mov_dpp(__builtin_bit_cast(int, x), CTRL, 0xf, 0xf, true));
}
constexpr int XOR1 = 0xB1, XOR2 = 0x4E, XOR7 = 0x141, XOR8 = 0x128, ROR4 = 0x124;
__device__ __forceinline__ float xrow16_max(float x) {
  auto s = __builtin_amdgcn_permlane16_swap(__float_as_uint(x), __float_as_uint(x), false, false);
  x = fmaxf(__uint_as_float(s[0]), __uint_as_float(s[1]));
  auto t = __builtin_amdgcn_permlane32_swap(__float_as_uint(x), __float_as_uint(x), false, false);
  return fmaxf(__uint_as_float(t[0]), __uint_as_float(t[1]));
}
__device__ __forceinline__ float xrow16_sum(float x) {
  auto s = __builtin_amdgcn_permlane16_swap(__float_as_uint(x), __float_as_uint(x), false, false);
  x = __uint_as_float(s[0]) + __uint_as_float(s[1]);
  auto t = __builtin_amdgcn_permlane32_swap(__float_as_uint(x), __float_as_uint(x), false, false);
  return __uint_as_float(t[0]) + __uint_as_float(t[1]);
}
constexpr int AUX_kbase = 18, AUX_vbase = 18;

constexpr int LDS_P = 0, P_PER_WAVE = NCH * 32;
constexpr int LDS_MRG = WAVES * P_PER_WAVE, MRG_REC = D_ + 4;
constexpr int LDS_TREE = LDS_MRG + WAVES * 2 * MRG_REC;
constexpr int LDS_FLOATS = LDS_TREE + 4;

__global__ void __launch_bounds__(NTHR)
attn_split_kernel(const unsigned short* __restrict__ q, const unsigned short* __restrict__ kc,
                  const unsigned short* __restrict__ vc, const unsigned short* __restrict__ kt,
                  const unsigned short* __restrict__ vt, const unsigned short* __restrict__ mask,
                  unsigned* epoch, unsigned long long* gran, unsigned short* __restrict__ out) {
  __shared__ __attribute__((aligned(16))) float lds[LDS_FLOATS];
  const int wg = blockIdx.x;
  const int grp = wg % (B_ * HKV);
  const int split = wg / (B_ * HKV);
  const int tid = threadIdx.x, lane = tid & 63;
  const int wave = __builtin_amdgcn_readfirstlane(tid >> 6);
  const int r = lane >> 4, c = lane & 15;

  const u32x4 q0 = *reinterpret_cast<const u32x4*>(q + (size_t)(grp * 2 + 0) * D_ + 8 * c);
  const u32x4 q1 = *reinterpret_cast<const u32x4*>(q + (size_t)(grp * 2 + 1) * D_ + 8 * c);
  const unsigned tag = __hip_atomic_load(epoch + grp * 32, __ATOMIC_RELAXED, __HIP_MEMORY_SCOPE_AGENT) + 1u;
  const u32x4 ktv = *reinterpret_cast<const u32x4*>(kt + (size_t)grp * D_ + 8 * c);
  const unsigned short vtd = vt[(size_t)grp * D_ + (tid & 127)];
  const unsigned short mkb = mask[grp / HKV];

  const size_t tok0 = (size_t)grp * C_ + (size_t)split * TPS + (size_t)wave * 16;
  constexpr size_t CH_STRIDE = (size_t)WAVES * 16 * D_;

  u32x4 buf[DEPTH][4];
  static_assert(NCH % DEPTH == 0 && NCH >= 2 * DEPTH, "ring depth divides the chunk count");
  const unsigned loff = (unsigned)lane * 16u;
  const char* kbase = reinterpret_cast<const char*>(kc + tok0 * D_);
  const char* vbase = reinterpret_cast<const char*>(vc + tok0 * D_);
  constexpr size_t CH_BYTES = CH_STRIDE * 2;
  const __amdgpu_buffer_rsrc_t rs_kbase = __builtin_amdgcn_make_buffer_rsrc((void*)kbase, (short)0, (int)((NCH - 1) * CH_STRIDE * 2 + 4096), 0x00020000);
  const __amdgpu_buffer_rsrc_t rs_vbase = __builtin_amdgcn_make_buffer_rsrc((void*)vbase, (short)0, (int)((NCH - 1) * CH_STRIDE * 2 + 4096), 0x00020000);
#define ISSUE(SLOT, BASE, CH) do { \
    _Pragma("unroll") for (int t_ = 0; t_ < 4; ++t_) \
      buf[SLOT][t_] = __builtin_bit_cast(u32x4, __builtin_amdgcn_raw_buffer_load_b128(rs_##BASE, loff + t_ * 1024, (int)((CH) * CH_BYTES), AUX_##BASE)); } while (0)
#pragma unroll
  for (int s = 0; s < DEPTH - 1; ++s) { __builtin_amdgcn_sched_barrier(0); ISSUE(s, kbase, s); }

  float* pw = lds + LDS_P + wave * P_PER_WAVE;
  float* pmine = pw + r * 8 + (c >> 1);
  float m = -INFINITY;
#define KSTAGE(U, CH) do { \
      float x[8]; \
      _Pragma("unroll") for (int t = 0; t < 4; ++t) { \
        const u32x4 kk = buf[U][t]; \
        float d0 = 0.f, d1 = 0.f; \
        _Pragma("unroll") for (int k = 0; k < 4; ++k) { d0 = dot2(kk[k], q0[k], d0); d1 = dot2(kk[k], q1[k], d1); } \
        x[t * 2 + 0] = d0; x[t * 2 + 1] = d1; \
      } \
      float y[4]; \
      _Pragma("unroll") for (int j = 0; j < 4; ++j) { \
        const float lo = x[j], hi = x[j + 4]; \
        const float a = lo + dpp<XOR8>(lo), b = hi + dpp<XOR8>(hi); \
        y[j] = (c & 8) ? b : a; \
      } \
      float z[2]; \
      _Pragma("unroll") for (int j = 0; j < 2; ++j) { \
        const float lo = y[j], hi = y[j + 2]; \
        const float a = lo + dpp<XOR7>(lo), b = hi + dpp<XOR7>(hi); \
        z[j] = (c & 4) ? b : a; \
      } \
      const float a_ = z[0] + dpp<XOR2>(z[0]), b_ = z[1] + dpp<XOR2>(z[1]); \
      float w = (c & 2) ? b_ : a_; \
      w += dpp<XOR1>(w); \
      w *= C2; \
      m = fmaxf(m, w); \
      pmine[(CH) * 32] = w; \
    } while (0)
#pragma unroll 1
  for (int s0 = 0; s0 < NCH - DEPTH; s0 += DEPTH) {
#pragma unroll
    for (int u = 0; u < DEPTH; ++u) {
      __builtin_amdgcn_sched_barrier(0);
      ISSUE((u + DEPTH - 1) % DEPTH, kbase, s0 + u + DEPTH - 1);
      __builtin_amdgcn_sched_barrier(0);
      KSTAGE(u, s0 + u);
    }
  }
#pragma unroll
  for (int u = 0; u < DEPTH; ++u) {
    __builtin_amdgcn_sched_barrier(0);
    if (u == 0) ISSUE(DEPTH - 1, kbase, NCH - 1); else ISSUE(u - 1, vbase, u - 1);
    __builtin_amdgcn_sched_barrier(0);
    KSTAGE(u, NCH - DEPTH + u);
  }
#undef KSTAGE
  if (wave == 0) {
    float d0 = 0.f, d1 = 0.f;
#pragma unroll
    for (int k = 0; k < 4; ++k) { d0 = dot2(ktv[k], q0[k], d0); d1 = dot2(ktv[k], q1[k], d1); }
    d0 += dpp<XOR1>(d0); d0 += dpp<XOR2>(d0); d0 += dpp<XOR7>(d0); d0 += dpp<XOR8>(d0);
    d1 += dpp<XOR1>(d1); d1 += dpp<XOR2>(d1); d1 += dpp<XOR7>(d1); d1 += dpp<XOR8>(d1);
    const float mk = __builtin_bit_cast(float, (unsigned)mkb << 16);
    const float madd = (1.0f - mk) * -1000000000.0f;
    if (lane == 0) { lds[LDS_TREE + 0] = (d0 * SCALE + madd) * LOG2E; lds[LDS_TREE + 1] = (d1 * SCALE + madd) * LOG2E; }
  }
  m = fmaxf(m, dpp<ROR4>(m));
  m = fmaxf(m, dpp<XOR8>(m));
  m = xrow16_max(m);
  float l = 0.f;
  {
    float* pp = pmine + (c & 1) * 32;
#pragma unroll
    for (int s = 0; s < NCH / 2; ++s) {
      const float p = __builtin_amdgcn_exp2f(pp[s * 64] - m);
      l += p;
      pp[s * 64] = p;
    }
  }
  l += dpp<XOR1>(l);
  l += dpp<ROR4>(l);
  l += dpp<XOR8>(l);
  l = xrow16_sum(l);
  __builtin_amdgcn_fence(__ATOMIC_RELEASE, "wavefront");
  __builtin_amdgcn_wave_barrier();
  __builtin_amdgcn_fence(__ATOMIC_ACQUIRE, "wavefront");

  f32x2 acc0[4], acc1[4];
#pragma unroll
  for (int k = 0; k < 4; ++k) { acc0[k] = f32x2{0.f, 0.f}; acc1[k] = f32x2{0.f, 0.f}; }
#define VSTAGE(U, CH) do { \
      const f32x4 pa = *reinterpret_cast<const f32x4*>(pw + (CH) * 32 + r * 8); \
      const f32x4 pb = *reinterpret_cast<const f32x4*>(pw + (CH) * 32 + r * 8 + 4); \
      _Pragma("unroll") for (int t = 0; t < 4; ++t) { \
        const u32x4 vv = buf[U][t]; \
        const float p0 = t == 0 ? pa[0] : t == 1 ? pa[2] : t == 2 ? pb[0] : pb[2]; \
        const float p1 = t == 0 ? pa[1] : t == 1 ? pa[3] : t == 2 ? pb[1] : pb[3]; \
        const f32x2 p0v = f32x2{p0, p0}, p1v = f32x2{p1, p1}; \
        _Pragma("unroll") for (int k = 0; k < 4; ++k) { \
          const f32x2 vf = f32x2{__builtin_bit_cast(float, vv[k] << 16), __builtin_bit_cast(float, vv[k] & 0xffff0000u)}; \
          acc0[k] = __builtin_elementwise_fma(vf, p0v, acc0[k]); \
          acc1[k] = __builtin_elementwise_fma(vf, p1v, acc1[k]); \
        } \
      } \
    } while (0)
#pragma unroll 1
  for (int j0 = 0; j0 < NCH - DEPTH; j0 += DEPTH) {
#pragma unroll
    for (int u = 0; u < DEPTH; ++u) {
      __builtin_amdgcn_sched_barrier(0);
      ISSUE((u + DEPTH - 1) % DEPTH, vbase, j0 + u + DEPTH - 1);
      __builtin_amdgcn_sched_barrier(0);
      VSTAGE(u, j0 + u);
    }
  }
  static_assert(NTHR == 2 * D_, "one thread per (head, dim)");
  const int h = wave >> 1, d = tid & 127;
  unsigned long long* gg = gran + (size_t)grp * NSPLIT * 2 * GREC;
  const unsigned long long* go = gg + (size_t)h * GREC + d;
  const unsigned long long* gs = gg + (size_t)((c >> 1) * 2 + h) * GREC + D_ + (c & 1);
  unsigned long long xo[NSPLIT], xs;
#define LOADG() do { \
    _Pragma("unroll") for (int s = 0; s < NSPLIT; ++s) \
      xo[s] = __hip_atomic_load(go + (size_t)s * 2 * GREC, __ATOMIC_RELAXED, __HIP_MEMORY_SCOPE_AGENT); \
    xs = __hip_atomic_load(gs, __ATOMIC_RELAXED, __HIP_MEMORY_SCOPE_AGENT); \
  } while (0)
#define BADG(BAD) do { \
    BAD = ((c >> 1) == split) ? 0u : ((unsigned)(xs >> 32) ^ tag); \
    _Pragma("unroll") for (int s = 0; s < NSPLIT; ++s) BAD |= (s == split) ? 0u : ((unsigned)(xo[s] >> 32) ^ tag); \
  } while (0)
#pragma unroll
  for (int u = 0; u < DEPTH; ++u) {
    __builtin_amdgcn_sched_barrier(0);
    if (u == 0) ISSUE(DEPTH - 1, vbase, NCH - 1);
    if (u == 0) LOADG();
    __builtin_amdgcn_sched_barrier(0);
    VSTAGE(u, NCH - DEPTH + u);
  }
#undef VSTAGE
#undef ISSUE
  float a0[8], a1[8];
#pragma unroll
  for (int k = 0; k < 4; ++k) {
    a0[2 * k] = xrow16_sum(acc0[k][0]); a0[2 * k + 1] = xrow16_sum(acc0[k][1]);
    a1[2 * k] = xrow16_sum(acc1[k][0]); a1[2 * k + 1] = xrow16_sum(acc1[k][1]);
  }
  float* mrg = lds + LDS_MRG;
  unsigned bad;
  BADG(bad);
  const bool wbad = __builtin_amdgcn_ballot_w64(bad != 0u) != 0ull;
  if (r == 0) {
    float* rec0 = mrg + (wave * 2 + 0) * MRG_REC;
    float* rec1 = mrg + (wave * 2 + 1) * MRG_REC;
    *reinterpret_cast<f32x4*>(rec0 + 4 + 8 * c) = f32x4{a0[0], a0[1], a0[2], a0[3]};
    *reinterpret_cast<f32x4*>(rec0 + 4 + 8 * c + 4) = f32x4{a0[4], a0[5], a0[6], a0[7]};
    *reinterpret_cast<f32x4*>(rec1 + 4 + 8 * c) = f32x4{a1[0], a1[1], a1[2], a1[3]};
    *reinterpret_cast<f32x4*>(rec1 + 4 + 8 * c + 4) = f32x4{a1[4], a1[5], a1[6], a1[7]};
    if (c == 0) { rec0[0] = m; rec0[1] = l; rec0[2] = wbad ? 1.f : 0.f; }
    if (c == 2) { rec1[0] = m; rec1[1] = l; }
  }
  __syncthreads();
  float Mo = -INFINITY, Lo = 0.f, oo = 0.f, anybad = 0.f;
#pragma unroll
  for (int w = 0; w < WAVES; ++w) { Mo = fmaxf(Mo, mrg[(w * 2 + h) * MRG_REC]); anybad += mrg[(w * 2) * MRG_REC + 2]; }
#pragma unroll
  for (int w = 0; w < WAVES; ++w) {
    const float* rec = mrg + (w * 2 + h) * MRG_REC;
    const float wt = __builtin_amdgcn_exp2f(rec[0] - Mo);
    Lo = fmaf(wt, rec[1], Lo);
    oo = fmaf(wt, rec[4 + d], oo);
  }
  if (anybad != 0.f) {
    const unsigned long long th = (unsigned long long)tag << 32;
    unsigned long long* g = gg + (size_t)(split * 2 + h) * GREC;
    __hip_atomic_store(g + d, th | __float_as_uint(oo), __ATOMIC_RELAXED, __HIP_MEMORY_SCOPE_AGENT);
    if (d < 2) __hip_atomic_store(g + D_ + d, th | __float_as_uint(d == 0 ? Mo : Lo), __ATOMIC_RELAXED, __HIP_MEMORY_SCOPE_AGENT);
    asm volatile("s_waitcnt vmcnt(0)" ::: "memory");
    __syncthreads();
    LOADG();
    BADG(bad);
    if (__syncthreads_or(bad != 0u ? 1 : 0)) return;
  }
  {
    const float st = lds[LDS_TREE + h];
    float mv[NSPLIT], lv[NSPLIT];
    const unsigned xsl = (unsigned)xs;
#pragma unroll
    for (int s = 0; s < NSPLIT; ++s) {
      const float ms = __uint_as_float(__builtin_amdgcn_readlane(xsl, 2 * s));
      const float ls = __uint_as_float(__builtin_amdgcn_readlane(xsl, 2 * s + 1));
      mv[s] = (s == split) ? Mo : ms; lv[s] = (s == split) ? Lo : ls;
    }
    float M = st;
#pragma unroll
    for (int s = 0; s < NSPLIT; ++s) M = fmaxf(M, mv[s]);
    const float wt = __builtin_amdgcn_exp2f(st - M);
    float L = wt, o = wt * __builtin_bit_cast(float, (unsigned)vtd << 16);
#pragma unroll
    for (int s = 0; s < NSPLIT; ++s) {
      const float w = __builtin_amdgcn_exp2f(mv[s] - M);
      const float os = (s == split) ? oo : __uint_as_float((unsigned)xo[s]);
      L = fmaf(w, lv[s], L); o = fmaf(w, os, o);
    }
    const float res = o * (1.0f / L);
    unsigned rr;
    asm volatile("v_cvt_pk_bf16_f32 %0, %1, %2" : "=v"(rr) : "v"(res), "v"(res));
    out[(size_t)(grp * 2 + h) * D_ + d] = (unsigned short)rr;
    if (tid == 0) __hip_atomic_store(epoch + grp * 32, tag, __ATOMIC_RELAXED, __HIP_MEMORY_SCOPE_AGENT);
  }
}

}

extern "C" void kernel_launch(void* const* d_in, const int* in_sizes, int n_in,
                              void* d_out, int out_size, void* d_ws, size_t ws_size,
                              hipStream_t stream) {
  const unsigned short* q = (const unsigned short*)d_in[0];
  const unsigned short* kc = (const unsigned short*)d_in[1];
  const unsigned short* vc = (const unsigned short*)d_in[2];
  const unsigned short* kt = (const unsigned short*)d_in[3];
  const unsigned short* vt = (const unsigned short*)d_in[4];
  const unsigned short* mask = (const unsigned short*)d_in[5];
  unsigned* epoch = (unsigned*)((char*)d_ws + WS_EPOCH);
  unsigned long long* gran = (unsigned long long*)((char*)d_ws + WS_GRAN);
  hipLaunchKernelGGL(attn_split_kernel, dim3(B_ * HKV * NSPLIT), dim3(NTHR), 0, stream, q, kc, vc, kt, vt, mask,
                     epoch, gran, (unsigned short*)d_out);
}
```
